```python
import torch

def _softmax(x, axis=-1):
    return torch.softmax(x, dim=axis)
def _gelu(x):
    return torch.nn.functional.gelu(x, approximate="tanh")
def _silu(x):
    return torch.nn.functional.silu(x)
def _sigmoid(x):
    return torch.sigmoid(x)
def _layernorm(x, axis=-1, eps=1e-5):
    m = x.mean(dim=axis, keepdim=True)
    v = x.var(dim=axis, keepdim=True, unbiased=False)
    return (x - m) / torch.sqrt(v + eps)
def _scatter_add(tgt, idx, src, axis=0):
    return tgt.clone().index_add_(axis, idx, src)
def _segment_sum(src, seg, n):
    out = torch.zeros((n,) + src.shape[1:], dtype=src.dtype)
    return out.index_add_(0, seg, src)
def _pad(x, pw):
    flat = tuple(v for pair in reversed(pw) for v in pair)
    return torch.nn.functional.pad(x, flat)

kernel_name = 'standalone__paged_block_table_kv_pagedattention__sampling'

def setup_inputs():

    _w = (lambda a: (lambda t: t.to(torch.bfloat16) if t.is_floating_point() else t)(torch.as_tensor(a)))
    B, Hq, Hkv, Sq, Skv, D = 16, 32, 8, 32, 4096, 128
    bs = 16
    nbps = max(1, Skv // bs)
    num_phys = B * nbps + 4
    bt = torch.randperm(num_phys)[: B * nbps].reshape(B, nbps).to(torch.int64)
    cl = (nbps * bs - (torch.arange(1, B + 1, dtype=torch.int64) * 2654435761) % max(1, nbps * bs // 2)).to(torch.int64)
    return {
        "q": _w((lambda *s: torch.randn(*s, dtype=torch.bfloat16) * 0.8)(B, Hq, Sq, D)),
        "k_cache": _w((lambda *s: torch.randn(*s, dtype=torch.bfloat16) * 0.8)(num_phys, bs, Hkv, D)),
        "v_cache": _w((lambda *s: torch.randn(*s, dtype=torch.bfloat16) * 0.05)(num_phys, bs, Hkv, D)),
        "block_table": _w(bt),
        "context_lens": _w(cl),
    }

def reference(q, k_cache, v_cache, block_table, context_lens):
    B, Hq, Sq, D = q.shape
    bs = k_cache.shape[1]
    Hkv = k_cache.shape[2]
    nbps = block_table.shape[1]
    Skv = nbps * bs
    bt = block_table.reshape(-1)
    k = (lambda x, *d: x.permute(*d))(k_cache[bt].reshape(B, Skv, Hkv, D), 0, 2, 1, 3)
    v = (lambda x, *d: x.permute(*d))(v_cache[bt].reshape(B, Skv, Hkv, D), 0, 2, 1, 3)
    g = Hq // Hkv
    qg = q.reshape(B, Hkv, g, Sq, D)
    scores = torch.einsum("bhgqd,bhkd->bhgqk", qg, k) * (D ** -0.5)
    pos = torch.arange(Skv)
    msk = pos[None, :] < context_lens[:, None]
    scores = torch.where(msk[:, None, None, None, :], scores, -1e9)
    p = _softmax(scores, axis=-1)
    out = torch.einsum("bhgqk,bhkd->bhgqd", p, v)
    return out.reshape(B, Hq, Sq, D)
```

```cpp
#include <hip/hip_runtime.h>
#include <hip/hip_bf16.h>
#include <stdint.h>

typedef __attribute__((ext_vector_type(8))) short bf16x8;
typedef __attribute__((ext_vector_type(4))) short s16x4;
typedef __attribute__((ext_vector_type(16))) float f32x16;
typedef __attribute__((ext_vector_type(4))) float f32x4;
typedef __attribute__((ext_vector_type(4))) unsigned u32x4;
typedef __attribute__((ext_vector_type(2))) unsigned u32x2;

constexpr int B = 16, HQ = 32, HKV = 8, SQ = 32, D = 128, BS = 16, NBPS = 256, SKV = NBPS * BS, G = HQ / HKV;
constexpr int KT = 64;
constexpr int NST = 4;
constexpr int NWG = 256;
constexpr int STAGE = 32768;
constexpr int NSLOT = NWG + B * HKV;
constexpr float SCALE_L2E = 0.088388347648318447f * 1.4426950408889634f;
constexpr float THR = 8.0f / (0.088388347648318447f * 1.4426950408889634f);

__device__ __forceinline__ int crow(int r, int h) { return (r & 3) + 8 * (r >> 2) + 4 * h; }
__device__ __forceinline__ unsigned cvtpk(float lo, float hi) {
  unsigned r; asm("v_cvt_pk_bf16_f32 %0, %1, %2" : "=v"(r) : "v"(lo), "v"(hi)); return r;
}
__device__ __forceinline__ int eff_len(int cl) { return (cl <= 0 || cl > SKV) ? SKV : cl; }

__device__ __forceinline__ void dma16(unsigned lds_addr, unsigned voff, const void* sbase) {
  unsigned keep;
  asm volatile("s_mov_b32 %0, m0\n\ts_mov_b32 m0, %1\n\ts_nop 0\n\tglobal_load_lds_dwordx4 %2, %3\n\ts_mov_b32 m0, %0"
               : "=&s"(keep) : "s"(lds_addr), "v"(voff), "s"(sbase) : "memory");
}

__global__ __launch_bounds__(512, 2) void attn_kernel(const __hip_bfloat16* __restrict__ q,
                                                      const __hip_bfloat16* __restrict__ kc,
                                                      const __hip_bfloat16* __restrict__ vc,
                                                      const int* __restrict__ bt, const int* __restrict__ cl,
                                                      float* __restrict__ po, float* __restrict__ pml) {
  extern __shared__ __attribute__((aligned(16))) char lds[];
  const int tid = threadIdx.x, lane = tid & 63;
  const int wid = __builtin_amdgcn_readfirstlane(tid >> 6);
  const int rg = wid & 3, kh = wid >> 2;
  const int l31 = lane & 31, h = lane >> 5;
  const int w = blockIdx.x;
  const unsigned ldsbase = (unsigned)(size_t)lds;

  int total = 0;
  for (int b = 0; b < B; ++b) total += (eff_len(cl[b]) + KT - 1) / KT;
  total *= HKV;
  int lo = (int)(((long)w * total) / NWG);
  const int hi = (int)(((long)(w + 1) * total) / NWG);

  unsigned dmaoff[2];
#pragma unroll
  for (int i = 0; i < 2; ++i) {
    const int f = ((lane >> 4) << 2) | ((2 * wid + i) & 3);
    dmaoff[i] = (unsigned)((((wid & 1) * 8 + 4 * i + (lane >> 4)) * (HKV * D * 2)) + 16 * ((lane & 15) ^ f));
  }
  const int krowi = kh * 32 + l31;
  const unsigned kbase = 256u * krowi + 16u * (unsigned)(h ^ (((krowi & 3) << 2) | ((krowi >> 2) & 3)));
  const int blk = (lane >> 4) & 1, qq = (lane & 15) >> 2, pp = lane & 3;

  int base = 0;
  for (int b = 0; b < B && lo < hi; ++b) {
    const int clb = cl[b];
    const int len = eff_len(clb);
    const bool allmask = clb <= 0;
    const int n = (len + KT - 1) / KT;
    while (lo < hi && lo < base + HKV * n) {
      const int kvh = (lo - base) / n;
      const int t0 = (lo - base) - kvh * n;
      const int t1 = min(n, t0 + (hi - lo));
      const int slot = w + b * HKV + kvh;
      lo += t1 - t0;

      const char* kbp = (const char*)kc + kvh * (D * 2);
      const char* vbp = (const char*)vc + kvh * (D * 2);
      const int* btp = bt + b * NBPS + (wid >> 1);

      auto issue = [&](int t, int st) {
        const int phys = btp[4 * t];
        const size_t pg = (size_t)phys * (BS * HKV * D * 2);
        const unsigned dst = ldsbase + st * STAGE + wid * 2048;
        dma16(dst, dmaoff[0], kbp + pg);
        dma16(dst + 1024, dmaoff[1], kbp + pg);
        dma16(dst + 16384, dmaoff[0], vbp + pg);
        dma16(dst + 16384 + 1024, dmaoff[1], vbp + pg);
      };

      const int nt = t1 - t0;
      issue(t0, 0);
      if (nt > 1) issue(t0 + 1, 1);
      if (nt > 2) issue(t0 + 2, 2);

      bf16x8 qf[8];
      {
        const __hip_bfloat16* qp = q + ((size_t)((b * HQ + kvh * G + rg) * SQ + l31)) * D + 8 * h;
#pragma unroll
        for (int d0 = 0; d0 < 8; ++d0) qf[d0] = *(const bf16x8*)(qp + 16 * d0);
      }
      f32x16 o[4];
#pragma unroll
      for (int c = 0; c < 4; ++c)
#pragma unroll
        for (int r = 0; r < 16; ++r) o[c][r] = 0.f;
      float m_run = -__builtin_inff(), l_run = 0.f;
#pragma unroll
      for (int d0 = 0; d0 < 8; ++d0) asm volatile("" ::"v"(qf[d0]));

      asm volatile("s_waitcnt vmcnt(0)" ::: "memory");
      __builtin_amdgcn_s_barrier();

      for (int t = t0; t < t1; ++t) {
        const int st = (t - t0) & 3;
        if (t + 3 < t1) issue(t + 3, (st + 3) & 3);
        const char* Ks = lds + st * STAGE;
        const char* Vs = Ks + 16384;

        f32x16 s;
#pragma unroll
        for (int r = 0; r < 16; ++r) s[r] = 0.f;
#pragma unroll
        for (int d0 = 0; d0 < 8; ++d0) {
          const bf16x8 kf = *(const bf16x8*)(Ks + (kbase ^ (unsigned)(d0 << 5)));
          s = __builtin_amdgcn_mfma_f32_32x32x16_bf16(kf, qf[d0], s, 0, 0, 0);
        }
        if (allmask) {
#pragma unroll
          for (int r = 0; r < 16; ++r) s[r] = 0.f;
        } else if ((t + 1) * KT > len) {
#pragma unroll
          for (int r = 0; r < 16; ++r) {
            const int key = t * KT + kh * 32 + crow(r, h);
            s[r] = key >= len ? -__builtin_inff() : s[r];
          }
        }
        float tmax = s[0];
#pragma unroll
        for (int r = 1; r < 16; ++r) tmax = fmaxf(tmax, s[r]);
        {
          auto rr = __builtin_amdgcn_permlane32_swap(__float_as_uint(tmax), __float_as_uint(tmax), false, false);
          tmax = fmaxf(__uint_as_float(rr[0]), __uint_as_float(rr[1]));
        }
        if (__any(tmax - m_run > THR)) {
          const float mn = fmaxf(m_run, tmax);
          const float alpha = (mn == -__builtin_inff()) ? 1.f : __builtin_amdgcn_exp2f((m_run - mn) * SCALE_L2E);
          m_run = mn;
          l_run *= alpha;
#pragma unroll
          for (int c = 0; c < 4; ++c)
#pragma unroll
            for (int r = 0; r < 16; ++r) o[c][r] *= alpha;
        }
        const float msub = (m_run == -__builtin_inff()) ? 0.f : m_run * SCALE_L2E;
        float psum = 0.f;
#pragma unroll
        for (int r = 0; r < 16; ++r) {
          s[r] = __builtin_amdgcn_exp2f(fmaf(s[r], SCALE_L2E, -msub));
          psum += s[r];
        }
        l_run += psum;
        bf16x8 pf[2];
#pragma unroll
        for (int ks = 0; ks < 2; ++ks) {
          u32x4 wv = {cvtpk(s[8 * ks + 0], s[8 * ks + 1]), cvtpk(s[8 * ks + 2], s[8 * ks + 3]),
                      cvtpk(s[8 * ks + 4], s[8 * ks + 5]), cvtpk(s[8 * ks + 6], s[8 * ks + 7])};
          pf[ks] = __builtin_bit_cast(bf16x8, wv);
        }
#pragma unroll
        for (int c = 0; c < 4; ++c) {
#pragma unroll
          for (int ks = 0; ks < 2; ++ks) {
            s16x4 vv[2];
#pragma unroll
            for (int tt = 0; tt < 2; ++tt) {
              const int row = kh * 32 + 16 * ks + 8 * tt + 4 * h + qq;
              const int ch = 4 * c + 2 * blk + (pp >> 1);
              const unsigned off = 256u * row + 16u * (unsigned)(ch ^ (((row & 3) << 2) | ((row >> 2) & 3))) + 8u * (pp & 1);
              vv[tt] = __builtin_amdgcn_ds_read_tr16_b64_v4i16(
                  (__attribute__((address_space(3))) s16x4*)(Vs + off));
            }
            const bf16x8 vf = __builtin_shufflevector(vv[0], vv[1], 0, 1, 2, 3, 4, 5, 6, 7);
            o[c] = __builtin_amdgcn_mfma_f32_32x32x16_bf16(vf, pf[ks], o[c], 0, 0, 0);
          }
        }
        const int rem = t1 - (t + 2);
        if (rem >= 2) asm volatile("s_waitcnt vmcnt(8)" ::: "memory");
        else if (rem == 1) asm volatile("s_waitcnt vmcnt(4)" ::: "memory");
        else asm volatile("s_waitcnt vmcnt(0)" ::: "memory");
        __builtin_amdgcn_s_barrier();
      }

      {
        auto rr = __builtin_amdgcn_permlane32_swap(__float_as_uint(l_run), __float_as_uint(l_run), false, false);
        l_run = __uint_as_float(rr[0]) + __uint_as_float(rr[1]);
      }
      float* lo_ = (float*)lds + rg * 4096;
      float* lst = (float*)(lds + 65536) + rg * 64;
      if (kh == 1) {
#pragma unroll
        for (int c = 0; c < 4; ++c)
#pragma unroll
          for (int g = 0; g < 4; ++g)
            *(f32x4*)(lo_ + ((c * 4 + g) * 64 + lane) * 4) =
                f32x4{o[c][4 * g], o[c][4 * g + 1], o[c][4 * g + 2], o[c][4 * g + 3]};
        if (h == 0) { lst[l31 * 2] = m_run; lst[l31 * 2 + 1] = l_run; }
      }
      __syncthreads();
      if (kh == 0) {
        const float m1 = lst[l31 * 2], l1 = lst[l31 * 2 + 1];
        const float mn = fmaxf(m_run, m1);
        const float a0 = (mn == -__builtin_inff()) ? 0.f : __builtin_amdgcn_exp2f((m_run - mn) * SCALE_L2E);
        const float a1 = (mn == -__builtin_inff()) ? 0.f : __builtin_amdgcn_exp2f((m1 - mn) * SCALE_L2E);
        float* pod = po + (size_t)slot * (4 * 4096) + rg * 4096;
#pragma unroll
        for (int c = 0; c < 4; ++c)
#pragma unroll
          for (int g = 0; g < 4; ++g) {
            const f32x4 x = *(const f32x4*)(lo_ + ((c * 4 + g) * 64 + lane) * 4);
            f32x4 y;
#pragma unroll
            for (int e = 0; e < 4; ++e) y[e] = o[c][4 * g + e] * a0 + x[e] * a1;
            *(f32x4*)(pod + ((c * 4 + g) * 64 + lane) * 4) = y;
          }
        if (h == 0) {
          float* pm = pml + ((size_t)slot * 4 + rg) * 64 + l31 * 2;
          pm[0] = mn; pm[1] = l_run * a0 + l1 * a1;
        }
      }
      __syncthreads();
    }
    base += HKV * n;
  }
}

__global__ __launch_bounds__(256) void combine_kernel(const int* __restrict__ cl, const float* __restrict__ po,
                                                      const float* __restrict__ pml, __hip_bfloat16* __restrict__ out) {
  const int s = blockIdx.x >> 2, rg = blockIdx.x & 3;
  const int b = s / HKV, kvh = s % HKV;
  const int tid = threadIdx.x, lane = tid & 63, c = tid >> 6;
  const int l31 = lane & 31, h = lane >> 5;
  int total = 0, base = 0, n = 0;
  for (int i = 0; i < B; ++i) {
    const int ni = (eff_len(cl[i]) + KT - 1) / KT;
    if (i < b) base += ni;
    if (i == b) n = ni;
    total += ni;
  }
  total *= HKV;
  base = base * HKV + kvh * n;
  const int x0 = base, x1 = base + n - 1;
  const int wf = (int)((((long)(x0 + 1) * NWG + total - 1) / total) - 1);
  const int wl = (int)((((long)(x1 + 1) * NWG + total - 1) / total) - 1);
  float M = -__builtin_inff();
  for (int w = wf; w <= wl; ++w) M = fmaxf(M, pml[((size_t)(w + s) * 4 + rg) * 64 + l31 * 2]);
  float L = 0.f;
  f32x4 acc[4];
#pragma unroll
  for (int g = 0; g < 4; ++g) acc[g] = f32x4{0.f, 0.f, 0.f, 0.f};
  for (int w = wf; w <= wl; ++w) {
    const float m = pml[((size_t)(w + s) * 4 + rg) * 64 + l31 * 2];
    const float l = pml[((size_t)(w + s) * 4 + rg) * 64 + l31 * 2 + 1];
    const float a = (m == -__builtin_inff()) ? 0.f : __builtin_amdgcn_exp2f((m - M) * SCALE_L2E);
    L += a * l;
    const float* p = po + (size_t)(w + s) * (4 * 4096) + rg * 4096;
#pragma unroll
    for (int g = 0; g < 4; ++g) {
      const f32x4 x = *(const f32x4*)(p + ((c * 4 + g) * 64 + lane) * 4);
#pragma unroll
      for (int e = 0; e < 4; ++e) acc[g][e] += a * x[e];
    }
  }
  const float inv = 1.f / L;
  __hip_bfloat16* op = out + ((size_t)((b * HQ + kvh * G + rg) * SQ + l31)) * D + 32 * c + 4 * h;
#pragma unroll
  for (int g = 0; g < 4; ++g) {
    u32x2 v = {cvtpk(acc[g][0] * inv, acc[g][1] * inv), cvtpk(acc[g][2] * inv, acc[g][3] * inv)};
    *(u32x2*)(op + 8 * g) = v;
  }
}

extern "C" void kernel_launch(void* const* d_in, const int* in_sizes, int n_in, void* d_out, int out_size, void* d_ws,
                              size_t ws_size, hipStream_t stream) {
  const __hip_bfloat16* q = (const __hip_bfloat16*)d_in[0];
  const __hip_bfloat16* kc = (const __hip_bfloat16*)d_in[1];
  const __hip_bfloat16* vc = (const __hip_bfloat16*)d_in[2];
  const int* bt = (const int*)d_in[3];
  const int* cl = (const int*)d_in[4];
  float* po = (float*)d_ws;
  float* pml = po + (size_t)NSLOT * 4 * 4096;
  const int shm = NST * STAGE;
  hipFuncSetAttribute((const void*)attn_kernel, hipFuncAttributeMaxDynamicSharedMemorySize, shm);
  attn_kernel<<<NWG, 512, shm, stream>>>(q, kc, vc, bt, cl, po, pml);
  combine_kernel<<<B * HKV * G, 256, 0, stream>>>(cl, po, pml, (__hip_bfloat16*)d_out);
}
```

```cpp
#include <hip/hip_runtime.h>
#include <hip/hip_bf16.h>
#include <stdint.h>

typedef __attribute__((ext_vector_type(8))) short bf16x8;
typedef __attribute__((ext_vector_type(4))) short s16x4;
typedef __attribute__((ext_vector_type(16))) float f32x16;
typedef __attribute__((ext_vector_type(4))) float f32x4;
typedef __attribute__((ext_vector_type(4))) unsigned u32x4;
typedef __attribute__((ext_vector_type(2))) unsigned u32x2;

constexpr int B = 16, HQ = 32, HKV = 8, SQ = 32, D = 128, BS = 16, NBPS = 256, SKV = NBPS * BS, G = HQ / HKV;
constexpr int KT = 64;
constexpr int NST = 4;
constexpr int NWG = 256;
constexpr int STAGE = 32768;
constexpr int NGRP = NWG / HKV;
constexpr int NSLOT = (NGRP + B) * HKV;
constexpr float SCALE_L2E = 0.088388347648318447f * 1.4426950408889634f;
constexpr float THR = 8.0f / (0.088388347648318447f * 1.4426950408889634f);

__device__ __forceinline__ int crow(int r, int h) { return (r & 3) + 8 * (r >> 2) + 4 * h; }
__device__ __forceinline__ unsigned cvtpk(float lo, float hi) {
  unsigned r; asm("v_cvt_pk_bf16_f32 %0, %1, %2" : "=v"(r) : "v"(lo), "v"(hi)); return r;
}
__device__ __forceinline__ int eff_len(int cl) { return (cl <= 0 || cl > SKV) ? SKV : cl; }

#define DMA_POLICY "nt"
__device__ __forceinline__ void dma16(unsigned lds_addr, unsigned voff, const void* sbase) {
  unsigned keep;
  asm volatile("s_mov_b32 %0, m0\n\ts_mov_b32 m0, %1\n\ts_nop 0\n\tglobal_load_lds_dwordx4 %2, %3 " DMA_POLICY "\n\ts_mov_b32 m0, %0"
               : "=&s"(keep) : "s"(lds_addr), "v"(voff), "s"(sbase) : "memory");
}

__device__ __forceinline__ void load_k(const char* Ks, unsigned kbase, bf16x8 (&kf)[8]) {
#pragma unroll
  for (int d0 = 0; d0 < 8; ++d0) kf[d0] = *(const bf16x8*)(Ks + (kbase ^ (unsigned)(d0 << 5)));
}
__device__ __forceinline__ void load_v(const char* Vs, const unsigned (&vbase)[2], bf16x8 (&vf)[8]) {
#pragma unroll
  for (int c = 0; c < 4; ++c)
#pragma unroll
    for (int ks = 0; ks < 2; ++ks) {
      const s16x4 a = __builtin_amdgcn_ds_read_tr16_b64_v4i16(
          (__attribute__((address_space(3))) s16x4*)(Vs + ((vbase[0] ^ (unsigned)(c << 6)) + 4096 * ks)));
      const s16x4 b = __builtin_amdgcn_ds_read_tr16_b64_v4i16(
          (__attribute__((address_space(3))) s16x4*)(Vs + ((vbase[1] ^ (unsigned)(c << 6)) + 4096 * ks)));
      vf[c * 2 + ks] = __builtin_shufflevector(a, b, 0, 1, 2, 3, 4, 5, 6, 7);
    }
}
__device__ __forceinline__ void qk_mfma(const bf16x8 (&kf)[8], const bf16x8 (&qf)[8], f32x16& s) {
#pragma unroll
  for (int r = 0; r < 16; ++r) s[r] = 0.f;
#pragma unroll
  for (int d0 = 0; d0 < 8; ++d0) s = __builtin_amdgcn_mfma_f32_32x32x16_bf16(kf[d0], qf[d0], s, 0, 0, 0);
}
__device__ __forceinline__ void pv_mfma(const bf16x8 (&vf)[8], const bf16x8 (&pf)[2], f32x16 (&o)[4]) {
#pragma unroll
  for (int ks = 0; ks < 2; ++ks)
#pragma unroll
    for (int c = 0; c < 4; ++c) o[c] = __builtin_amdgcn_mfma_f32_32x32x16_bf16(vf[c * 2 + ks], pf[ks], o[c], 0, 0, 0);
}
__device__ __forceinline__ void softmax_tile(f32x16& s, bool masked, int key0, int lim, f32x16 (&o)[4], float& m_run,
                                             float& l_run, bf16x8 (&pf)[2]) {
  if (masked) {
#pragma unroll
    for (int r = 0; r < 16; ++r) s[r] = (key0 + (r & 3) + 8 * (r >> 2) >= lim) ? -__builtin_inff() : s[r];
  }
  float tmax = s[0];
#pragma unroll
  for (int r = 1; r < 16; ++r) tmax = fmaxf(tmax, s[r]);
  {
    auto rr = __builtin_amdgcn_permlane32_swap(__float_as_uint(tmax), __float_as_uint(tmax), false, false);
    tmax = fmaxf(__uint_as_float(rr[0]), __uint_as_float(rr[1]));
  }
  if (__any(tmax - m_run > THR)) {
    const float mn = fmaxf(m_run, tmax);
    const float alpha = (mn == -__builtin_inff()) ? 1.f : __builtin_amdgcn_exp2f((m_run - mn) * SCALE_L2E);
    m_run = mn;
    l_run *= alpha;
#pragma unroll
    for (int c = 0; c < 4; ++c)
#pragma unroll
      for (int r = 0; r < 16; ++r) o[c][r] *= alpha;
  }
  const float msub = (m_run == -__builtin_inff()) ? 0.f : m_run * SCALE_L2E;
  float psum = 0.f;
#pragma unroll
  for (int r = 0; r < 16; ++r) {
    s[r] = __builtin_amdgcn_exp2f(fmaf(s[r], SCALE_L2E, -msub));
    psum += s[r];
  }
  l_run += psum;
#pragma unroll
  for (int ks = 0; ks < 2; ++ks) {
    u32x4 wv = {cvtpk(s[8 * ks + 0], s[8 * ks + 1]), cvtpk(s[8 * ks + 2], s[8 * ks + 3]),
                cvtpk(s[8 * ks + 4], s[8 * ks + 5]), cvtpk(s[8 * ks + 6], s[8 * ks + 7])};
    pf[ks] = __builtin_bit_cast(bf16x8, wv);
  }
}

__global__ __launch_bounds__(512, 2) void attn_kernel(const __hip_bfloat16* __restrict__ q,
                                                      const __hip_bfloat16* __restrict__ kc,
                                                      const __hip_bfloat16* __restrict__ vc,
                                                      const int* __restrict__ bt, const int* __restrict__ cl,
                                                      float* __restrict__ po, float* __restrict__ pml) {
  extern __shared__ __attribute__((aligned(16))) char lds[];
  const int tid = threadIdx.x, lane = tid & 63;
  const int wid = __builtin_amdgcn_readfirstlane(tid >> 6);
  const int rg = wid & 3, kh = wid >> 2;
  const int l31 = lane & 31, h = lane >> 5;
  const int w = blockIdx.x;
  const unsigned ldsbase = (unsigned)(size_t)lds;

  int total = 0;
  for (int b = 0; b < B; ++b) total += (eff_len(cl[b]) + KT - 1) / KT;
  const int grp = w >> 3, kvh = w & 7;
  int lo = (grp * total) / NGRP;
  const int hi = ((grp + 1) * total) / NGRP;

  unsigned dmaoff[2];
#pragma unroll
  for (int i = 0; i < 2; ++i) {
    const int f = ((lane >> 4) << 2) | ((2 * wid + i) & 3);
    dmaoff[i] = (unsigned)((((wid & 1) * 8 + 4 * i + (lane >> 4)) * (HKV * D * 2)) + 16 * ((lane & 15) ^ f));
  }
  const int krowi = kh * 32 + l31;
  const unsigned kbase = 256u * krowi + 16u * (unsigned)(h ^ (((krowi & 3) << 2) | ((krowi >> 2) & 3)));
  unsigned vbase[2];
  {
    const int blk = (lane >> 4) & 1, qq = (lane & 15) >> 2, pp = lane & 3;
#pragma unroll
    for (int tt = 0; tt < 2; ++tt) {
      const int row = kh * 32 + 8 * tt + 4 * h + qq;
      const int ch = 2 * blk + (pp >> 1);
      vbase[tt] = 256u * row + 16u * (unsigned)(ch ^ (((row & 3) << 2) | ((row >> 2) & 3))) + 8u * (pp & 1);
    }
  }

  int base = 0;
  for (int b = 0; b < B && lo < hi; ++b) {
    const int clb = cl[b];
    const int len = eff_len(clb);
    const bool allmask = clb <= 0;
    const int n = (len + KT - 1) / KT;
    if (lo < base + n) {
      const int t0 = lo - base;
      const int t1 = min(n, t0 + (hi - lo));
      const int slot = (grp + b) * HKV + kvh;
      lo += t1 - t0;

      const char* kbp = (const char*)kc + kvh * (D * 2);
      const char* vbp = (const char*)vc + kvh * (D * 2);
      const int* btp = bt + b * NBPS + (wid >> 1);

      auto issueK = [&](int t) {
        const int phys = btp[4 * t];
        const size_t pg = (size_t)phys * (BS * HKV * D * 2);
        const unsigned dst = ldsbase + ((t - t0) & 3) * STAGE + wid * 2048;
        dma16(dst, dmaoff[0], kbp + pg);
        dma16(dst + 1024, dmaoff[1], kbp + pg);
      };
      auto issueV = [&](int t) {
        const int phys = btp[4 * t];
        const size_t pg = (size_t)phys * (BS * HKV * D * 2);
        const unsigned dst = ldsbase + ((t - t0) & 3) * STAGE + wid * 2048 + 16384;
        dma16(dst, dmaoff[0], vbp + pg);
        dma16(dst + 1024, dmaoff[1], vbp + pg);
      };

      const int nt = t1 - t0;
      issueK(t0); issueV(t0);
      if (nt > 1) { issueK(t0 + 1); issueV(t0 + 1); }
      if (nt > 2) issueK(t0 + 2);

      bf16x8 qf[8];
      {
        const __hip_bfloat16* qp = q + ((size_t)((b * HQ + kvh * G + rg) * SQ + l31)) * D + 8 * h;
#pragma unroll
        for (int d0 = 0; d0 < 8; ++d0) qf[d0] = *(const bf16x8*)(qp + 16 * d0);
        if (allmask) {
#pragma unroll
          for (int d0 = 0; d0 < 8; ++d0) qf[d0] = bf16x8{0, 0, 0, 0, 0, 0, 0, 0};
        }
      }
      f32x16 o[4];
#pragma unroll
      for (int c = 0; c < 4; ++c)
#pragma unroll
        for (int r = 0; r < 16; ++r) o[c][r] = 0.f;
      float m_run = -__builtin_inff(), l_run = 0.f;
#pragma unroll
      for (int d0 = 0; d0 < 8; ++d0) asm volatile("" ::"v"(qf[d0]));

      asm volatile("s_waitcnt vmcnt(0)" ::: "memory");
      __builtin_amdgcn_s_barrier();
      const int lim = allmask ? SKV : len;
      const int mtile = (lim + KT - 1) / KT - 1;
      const int key0 = mtile * KT + kh * 32 + 4 * h;
      f32x16 s;
      bf16x8 pf[2];
      bf16x8 kf[8], vf[8];

      auto pre = [&](int k) {
        if (k + 3 < t1) issueK(k + 3);
        if (k + 2 < t1) issueV(k + 2);
      };
      auto post = [&](int k) {
        if (k + 3 < t1) asm volatile("s_waitcnt vmcnt(8)" ::: "memory");
        else if (k + 2 < t1) asm volatile("s_waitcnt vmcnt(6)" ::: "memory");
        else asm volatile("s_waitcnt vmcnt(0)" ::: "memory");
        __builtin_amdgcn_s_barrier();
      };
#define KST(k) (lds + (((k) - t0) & 3) * STAGE)
#define VST(k) (lds + (((k) - t0) & 3) * STAGE + 16384)
      if (kh == 0) {
        pre(t0);
        load_k(KST(t0), kbase, kf);
        qk_mfma(kf, qf, s);
        softmax_tile(s, t0 == mtile, key0, lim, o, m_run, l_run, pf);
        post(t0);
        for (int k = t0 + 1; k < t1; ++k) {
          pre(k);
          load_v(VST(k - 1), vbase, vf);
          load_k(KST(k), kbase, kf);
          __builtin_amdgcn_sched_barrier(0);
          pv_mfma(vf, pf, o);
          qk_mfma(kf, qf, s);
          softmax_tile(s, k == mtile, key0, lim, o, m_run, l_run, pf);
          post(k);
        }
        load_v(VST(t1 - 1), vbase, vf);
        pv_mfma(vf, pf, o);
      } else {
        pre(t0);
        load_k(KST(t0), kbase, kf);
        qk_mfma(kf, qf, s);
        post(t0);
        for (int k = t0 + 1; k < t1; ++k) {
          pre(k);
          load_v(VST(k - 1), vbase, vf);
          load_k(KST(k), kbase, kf);
          __builtin_amdgcn_sched_barrier(0);
          softmax_tile(s, false, key0, lim, o, m_run, l_run, pf);
          __builtin_amdgcn_sched_barrier(0);
          pv_mfma(vf, pf, o);
          qk_mfma(kf, qf, s);
          post(k);
        }
        load_v(VST(t1 - 1), vbase, vf);
        softmax_tile(s, t1 - 1 == mtile, key0, lim, o, m_run, l_run, pf);
        pv_mfma(vf, pf, o);
      }
      __builtin_amdgcn_s_barrier();

      {
        auto rr = __builtin_amdgcn_permlane32_swap(__float_as_uint(l_run), __float_as_uint(l_run), false, false);
        l_run = __uint_as_float(rr[0]) + __uint_as_float(rr[1]);
      }
      float* lo_ = (float*)lds + rg * 4096;
      float* lst = (float*)(lds + 65536) + rg * 64;
      if (kh == 1) {
#pragma unroll
        for (int c = 0; c < 4; ++c)
#pragma unroll
          for (int g = 0; g < 4; ++g)
            *(f32x4*)(lo_ + ((c * 4 + g) * 64 + lane) * 4) =
                f32x4{o[c][4 * g], o[c][4 * g + 1], o[c][4 * g + 2], o[c][4 * g + 3]};
        if (h == 0) { lst[l31 * 2] = m_run; lst[l31 * 2 + 1] = l_run; }
      }
      __syncthreads();
      if (kh == 0) {
        const float m1 = lst[l31 * 2], l1 = lst[l31 * 2 + 1];
        const float mn = fmaxf(m_run, m1);
        const float a0 = (mn == -__builtin_inff()) ? 0.f : __builtin_amdgcn_exp2f((m_run - mn) * SCALE_L2E);
        const float a1 = (mn == -__builtin_inff()) ? 0.f : __builtin_amdgcn_exp2f((m1 - mn) * SCALE_L2E);
        float* pod = po + (size_t)slot * (4 * 4096) + rg * 4096;
#pragma unroll
        for (int c = 0; c < 4; ++c)
#pragma unroll
          for (int g = 0; g < 4; ++g) {
            const f32x4 x = *(const f32x4*)(lo_ + ((c * 4 + g) * 64 + lane) * 4);
            f32x4 y;
#pragma unroll
            for (int e = 0; e < 4; ++e) y[e] = o[c][4 * g + e] * a0 + x[e] * a1;
            *(f32x4*)(pod + ((c * 4 + g) * 64 + lane) * 4) = y;
          }
        if (h == 0) {
          float* pm = pml + ((size_t)slot * 4 + rg) * 64 + l31 * 2;
          pm[0] = mn; pm[1] = l_run * a0 + l1 * a1;
        }
      }
      __syncthreads();
    }
    base += n;
  }
}

__global__ __launch_bounds__(256) void combine_kernel(const int* __restrict__ cl, const float* __restrict__ po,
                                                      const float* __restrict__ pml, __hip_bfloat16* __restrict__ out) {
  const int s = blockIdx.x >> 2, rg = blockIdx.x & 3;
  const int b = s / HKV, kvh = s % HKV;
  const int tid = threadIdx.x, lane = tid & 63, c = tid >> 6;
  const int l31 = lane & 31, h = lane >> 5;
  int total = 0, base = 0, n = 0;
  for (int i = 0; i < B; ++i) {
    const int ni = (eff_len(cl[i]) + KT - 1) / KT;
    if (i < b) base += ni;
    if (i == b) n = ni;
    total += ni;
  }
  const int x0 = base, x1 = base + n - 1;
  const int wf = ((x0 + 1) * NGRP + total - 1) / total - 1;
  const int wl = ((x1 + 1) * NGRP + total - 1) / total - 1;
  float M = -__builtin_inff();
  for (int w = wf; w <= wl; ++w) M = fmaxf(M, pml[((size_t)((w + b) * HKV + kvh) * 4 + rg) * 64 + l31 * 2]);
  float L = 0.f;
  f32x4 acc[4];
#pragma unroll
  for (int g = 0; g < 4; ++g) acc[g] = f32x4{0.f, 0.f, 0.f, 0.f};
  for (int w = wf; w <= wl; ++w) {
    const int slot = (w + b) * HKV + kvh;
    const float m = pml[((size_t)slot * 4 + rg) * 64 + l31 * 2];
    const float l = pml[((size_t)slot * 4 + rg) * 64 + l31 * 2 + 1];
    const float a = (m == -__builtin_inff()) ? 0.f : __builtin_amdgcn_exp2f((m - M) * SCALE_L2E);
    L += a * l;
    const float* p = po + (size_t)slot * (4 * 4096) + rg * 4096;
#pragma unroll
    for (int g = 0; g < 4; ++g) {
      const f32x4 x = *(const f32x4*)(p + ((c * 4 + g) * 64 + lane) * 4);
#pragma unroll
      for (int e = 0; e < 4; ++e) acc[g][e] += a * x[e];
    }
  }
  const float inv = 1.f / L;
  __hip_bfloat16* op = out + ((size_t)((b * HQ + kvh * G + rg) * SQ + l31)) * D + 32 * c + 4 * h;
#pragma unroll
  for (int g = 0; g < 4; ++g) {
    u32x2 v = {cvtpk(acc[g][0] * inv, acc[g][1] * inv), cvtpk(acc[g][2] * inv, acc[g][3] * inv)};
    *(u32x2*)(op + 8 * g) = v;
  }
}

extern "C" void kernel_launch(void* const* d_in, const int* in_sizes, int n_in, void* d_out, int out_size, void* d_ws,
                              size_t ws_size, hipStream_t stream) {
  const __hip_bfloat16* q = (const __hip_bfloat16*)d_in[0];
  const __hip_bfloat16* kc = (const __hip_bfloat16*)d_in[1];
  const __hip_bfloat16* vc = (const __hip_bfloat16*)d_in[2];
  const int* bt = (const int*)d_in[3];
  const int* cl = (const int*)d_in[4];
  float* po = (float*)d_ws;
  float* pml = po + (size_t)NSLOT * 4 * 4096;
  const int shm = NST * STAGE;
  hipFuncSetAttribute((const void*)attn_kernel, hipFuncAttributeMaxDynamicSharedMemorySize, shm);
  attn_kernel<<<NWG, 512, shm, stream>>>(q, kc, vc, bt, cl, po, pml);
  combine_kernel<<<B * HKV * G, 256, 0, stream>>>(cl, po, pml, (__hip_bfloat16*)d_out);
}
```

```cpp
#include <hip/hip_runtime.h>
#include <hip/hip_bf16.h>
#include <stdint.h>

typedef __attribute__((ext_vector_type(8))) short bf16x8;
typedef __attribute__((ext_vector_type(4))) short s16x4;
typedef __attribute__((ext_vector_type(16))) float f32x16;
typedef __attribute__((ext_vector_type(4))) float f32x4;
typedef __attribute__((ext_vector_type(4))) unsigned u32x4;
typedef __attribute__((ext_vector_type(2))) unsigned u32x2;

constexpr int B = 16, HQ = 32, HKV = 8, SQ = 32, D = 128, BS = 16, NBPS = 256, SKV = NBPS * BS, G = HQ / HKV;
constexpr int KT = 64;
constexpr int NST = 4;
constexpr int NWG = 256;
constexpr int STAGE = 32768;
constexpr int NGRP = NWG / HKV;
constexpr int NSLOT = (NGRP + B) * HKV;
constexpr float SCALE_L2E = 0.088388347648318447f * 1.4426950408889634f;
constexpr float THR = 8.0f / (0.088388347648318447f * 1.4426950408889634f);

__device__ __forceinline__ int crow(int r, int h) { return (r & 3) + 8 * (r >> 2) + 4 * h; }
__device__ __forceinline__ unsigned cvtpk(float lo, float hi) {
  unsigned r; asm("v_cvt_pk_bf16_f32 %0, %1, %2" : "=v"(r) : "v"(lo), "v"(hi)); return r;
}
__device__ __forceinline__ int eff_len(int cl) { return (cl <= 0 || cl > SKV) ? SKV : cl; }

#define DMA_POLICY "nt"
__device__ __forceinline__ void dma16(unsigned lds_addr, unsigned voff, const void* sbase) {
  unsigned keep;
  asm volatile("s_mov_b32 %0, m0\n\ts_mov_b32 m0, %1\n\ts_nop 0\n\tglobal_load_lds_dwordx4 %2, %3 " DMA_POLICY "\n\ts_mov_b32 m0, %0"
               : "=&s"(keep) : "s"(lds_addr), "v"(voff), "s"(sbase) : "memory");
}

__device__ __forceinline__ void load_k(const char* Ks, unsigned kbase, bf16x8 (&kf)[8]) {
#pragma unroll
  for (int d0 = 0; d0 < 8; ++d0) kf[d0] = *(const bf16x8*)(Ks + (kbase ^ (unsigned)(d0 << 5)));
}
__device__ __forceinline__ void load_v(const char* Vs, const unsigned (&vbase)[2], bf16x8 (&vf)[8]) {
#pragma unroll
  for (int c = 0; c < 4; ++c)
#pragma unroll
    for (int ks = 0; ks < 2; ++ks) {
      const s16x4 a = __builtin_amdgcn_ds_read_tr16_b64_v4i16(
          (__attribute__((address_space(3))) s16x4*)(Vs + ((vbase[0] ^ (unsigned)(c << 6)) + 4096 * ks)));
      const s16x4 b = __builtin_amdgcn_ds_read_tr16_b64_v4i16(
          (__attribute__((address_space(3))) s16x4*)(Vs + ((vbase[1] ^ (unsigned)(c << 6)) + 4096 * ks)));
      vf[c * 2 + ks] = __builtin_shufflevector(a, b, 0, 1, 2, 3, 4, 5, 6, 7);
    }
}
__device__ __forceinline__ void qk_mfma(const bf16x8 (&kf)[8], const bf16x8 (&qf)[8], f32x16& s) {
#pragma unroll
  for (int r = 0; r < 16; ++r) s[r] = 0.f;
#pragma unroll
  for (int d0 = 0; d0 < 8; ++d0) s = __builtin_amdgcn_mfma_f32_32x32x16_bf16(kf[d0], qf[d0], s, 0, 0, 0);
}
__device__ __forceinline__ void pv_mfma(const bf16x8 (&vf)[8], const bf16x8 (&pf)[2], f32x16 (&o)[4]) {
#pragma unroll
  for (int ks = 0; ks < 2; ++ks)
#pragma unroll
    for (int c = 0; c < 4; ++c) o[c] = __builtin_amdgcn_mfma_f32_32x32x16_bf16(vf[c * 2 + ks], pf[ks], o[c], 0, 0, 0);
}
__device__ __forceinline__ void softmax_tile(f32x16& s, bool masked, int key0, int lim, f32x16 (&o)[4], float& m_run,
                                             float& l_run, bf16x8 (&pf)[2]) {
  if (masked) {
#pragma unroll
    for (int r = 0; r < 16; ++r) s[r] = (key0 + (r & 3) + 8 * (r >> 2) >= lim) ? -__builtin_inff() : s[r];
  }
  float tmax = s[0];
#pragma unroll
  for (int r = 1; r < 16; ++r) tmax = fmaxf(tmax, s[r]);
  {
    auto rr = __builtin_amdgcn_permlane32_swap(__float_as_uint(tmax), __float_as_uint(tmax), false, false);
    tmax = fmaxf(__uint_as_float(rr[0]), __uint_as_float(rr[1]));
  }
  if (__any(tmax - m_run > THR)) {
    const float mn = fmaxf(m_run, tmax);
    const float alpha = (mn == -__builtin_inff()) ? 1.f : __builtin_amdgcn_exp2f((m_run - mn) * SCALE_L2E);
    m_run = mn;
    l_run *= alpha;
#pragma unroll
    for (int c = 0; c < 4; ++c)
#pragma unroll
      for (int r = 0; r < 16; ++r) o[c][r] *= alpha;
  }
  const float msub = (m_run == -__builtin_inff()) ? 0.f : m_run * SCALE_L2E;
  float psum = 0.f;
#pragma unroll
  for (int r = 0; r < 16; ++r) {
    s[r] = __builtin_amdgcn_exp2f(fmaf(s[r], SCALE_L2E, -msub));
    psum += s[r];
  }
  l_run += psum;
#pragma unroll
  for (int ks = 0; ks < 2; ++ks) {
    u32x4 wv = {cvtpk(s[8 * ks + 0], s[8 * ks + 1]), cvtpk(s[8 * ks + 2], s[8 * ks + 3]),
                cvtpk(s[8 * ks + 4], s[8 * ks + 5]), cvtpk(s[8 * ks + 6], s[8 * ks + 7])};
    pf[ks] = __builtin_bit_cast(bf16x8, wv);
  }
}

__global__ __launch_bounds__(512, 2) void attn_kernel(const __hip_bfloat16* __restrict__ q,
                                                      const __hip_bfloat16* __restrict__ kc,
                                                      const __hip_bfloat16* __restrict__ vc,
                                                      const int* __restrict__ bt, const int* __restrict__ cl,
                                                      float* __restrict__ po, float* __restrict__ pml) {
  extern __shared__ __attribute__((aligned(16))) char lds[];
  const int tid = threadIdx.x, lane = tid & 63;
  const int wid = __builtin_amdgcn_readfirstlane(tid >> 6);
  const int rg = wid & 3, kh = wid >> 2;
  const int l31 = lane & 31, h = lane >> 5;
  const int w = blockIdx.x;
  const unsigned ldsbase = (unsigned)(size_t)lds;

  dma16(ldsbase + NST * STAGE + wid * 2048, (unsigned)(wid * 2048 + lane * 16), bt);
  dma16(ldsbase + NST * STAGE + wid * 2048 + 1024, (unsigned)(wid * 2048 + 1024 + lane * 16), bt);

  int total = 0;
  for (int b = 0; b < B; ++b) total += (eff_len(cl[b]) + KT - 1) / KT;
  const int grp = w >> 3, kvh = w & 7;
  int lo = (grp * total) / NGRP;
  const int hi = ((grp + 1) * total) / NGRP;

  unsigned dmaoff[2];
#pragma unroll
  for (int i = 0; i < 2; ++i) {
    const int f = ((lane >> 4) << 2) | ((2 * wid + i) & 3);
    dmaoff[i] = (unsigned)((((wid & 1) * 8 + 4 * i + (lane >> 4)) * (HKV * D * 2)) + 16 * ((lane & 15) ^ f));
  }
  const int krowi = kh * 32 + l31;
  const unsigned kbase = 256u * krowi + 16u * (unsigned)(h ^ (((krowi & 3) << 2) | ((krowi >> 2) & 3)));
  unsigned vbase[2];
  {
    const int blk = (lane >> 4) & 1, qq = (lane & 15) >> 2, pp = lane & 3;
#pragma unroll
    for (int tt = 0; tt < 2; ++tt) {
      const int row = kh * 32 + 8 * tt + 4 * h + qq;
      const int ch = 2 * blk + (pp >> 1);
      vbase[tt] = 256u * row + 16u * (unsigned)(ch ^ (((row & 3) << 2) | ((row >> 2) & 3))) + 8u * (pp & 1);
    }
  }

  const int N = hi - lo;
  if (N <= 0) return;
  int mypage;
  {
    const int pos = lo + lane;
    int bsel = 0, bbase = 0, base = 0;
    for (int b = 0; b < B; ++b) {
      const int n = (eff_len(cl[b]) + KT - 1) / KT;
      base += n;
      if (pos >= base && b + 1 < B) { bsel = b + 1; bbase = base; }
    }
    asm volatile("s_waitcnt vmcnt(0)" ::: "memory");
    __builtin_amdgcn_s_barrier();
    mypage = ((const int*)(lds + NST * STAGE))[bsel * NBPS + 4 * min(pos - bbase, SKV / KT - 1) + (wid >> 1)];
  }
  const char* kbp = (const char*)kc + kvh * (D * 2);
  const char* vbp = (const char*)vc + kvh * (D * 2);
  auto issueK = [&](int p) {
    const int phys = __builtin_amdgcn_readlane(mypage, p);
    const size_t pg = (size_t)phys * (BS * HKV * D * 2);
    const unsigned dst = ldsbase + (p & 3) * STAGE + wid * 2048;
    dma16(dst, dmaoff[0], kbp + pg);
    dma16(dst + 1024, dmaoff[1], kbp + pg);
  };
  auto issueV = [&](int p) {
    const int phys = __builtin_amdgcn_readlane(mypage, p);
    const size_t pg = (size_t)phys * (BS * HKV * D * 2);
    const unsigned dst = ldsbase + (p & 3) * STAGE + wid * 2048 + 16384;
    dma16(dst, dmaoff[0], vbp + pg);
    dma16(dst + 1024, dmaoff[1], vbp + pg);
  };
  auto pre = [&](int p) {
    if (p + 3 < N) issueK(p + 3);
    if (p + 2 < N) issueV(p + 2);
  };
  auto post = [&](int p) {
    if (p + 3 < N) asm volatile("s_waitcnt vmcnt(8)" ::: "memory");
    else if (p + 2 < N) asm volatile("s_waitcnt vmcnt(6)" ::: "memory");
    else asm volatile("s_waitcnt vmcnt(0)" ::: "memory");
    __builtin_amdgcn_s_barrier();
  };
#define KST(p) (lds + ((p) & 3) * STAGE)
#define VST(p) (lds + ((p) & 3) * STAGE + 16384)
#define BAR() do { asm volatile("s_waitcnt lgkmcnt(0)" ::: "memory"); __builtin_amdgcn_s_barrier(); } while (0)

  bf16x8 qf[8];
  auto load_q_plain = [&](int b) {
    const __hip_bfloat16* qp = q + ((size_t)((b * HQ + kvh * G + rg) * SQ + l31)) * D + 8 * h;
#pragma unroll
    for (int d0 = 0; d0 < 8; ++d0) qf[d0] = *(const bf16x8*)(qp + 16 * d0);
  };
  auto load_q_issue = [&](int b) {
    const __hip_bfloat16* qp = q + ((size_t)((b * HQ + kvh * G + rg) * SQ + l31)) * D + 8 * h;
#pragma unroll
    for (int d0 = 0; d0 < 8; ++d0)
      asm volatile("global_load_dwordx4 %0, %1, off offset:%2" : "+v"(qf[d0]) : "v"(qp), "i"(32 * d0) : "memory");
  };
  auto load_q_wait = [&]() {
    asm volatile("s_waitcnt vmcnt(0)"
                 : "+v"(qf[0]), "+v"(qf[1]), "+v"(qf[2]), "+v"(qf[3]), "+v"(qf[4]), "+v"(qf[5]), "+v"(qf[6]), "+v"(qf[7])
                 :: "memory");
  };

  int first_b = 0;
  {
    int base = 0;
    for (int b = 0; b < B; ++b) { base += (eff_len(cl[b]) + KT - 1) / KT; if (lo >= base) first_b = b + 1; }
  }
  issueK(0); issueV(0);
  if (N > 1) { issueK(1); issueV(1); }
  if (N > 2) issueK(2);
  load_q_plain(first_b);

  int base = 0, pos = lo;
  for (int b = 0; b < B && pos < hi; ++b) {
    const int clb = cl[b];
    const int len = eff_len(clb);
    const bool allmask = clb <= 0;
    const int n = (len + KT - 1) / KT;
    if (pos < base + n) {
      const int t0 = pos - base;
      const int t1 = min(n, t0 + (hi - pos));
      const int slot = (grp + b) * HKV + kvh;
      const int p0 = pos - lo, p1 = p0 + (t1 - t0);
      pos += t1 - t0;

#pragma unroll
      for (int d0 = 0; d0 < 8; ++d0) asm volatile("" ::"v"(qf[d0]));
      asm volatile("s_waitcnt vmcnt(0)" ::: "memory");
      __builtin_amdgcn_s_barrier();
      if (allmask) {
#pragma unroll
        for (int d0 = 0; d0 < 8; ++d0) qf[d0] = bf16x8{0, 0, 0, 0, 0, 0, 0, 0};
      }
      f32x16 o[4];
#pragma unroll
      for (int c = 0; c < 4; ++c)
#pragma unroll
        for (int r = 0; r < 16; ++r) o[c][r] = 0.f;
      float m_run = -__builtin_inff(), l_run = 0.f;
      const int pm = p0 + (n - 1 - t0);
      const int key0 = (n - 1) * KT + kh * 32 + 4 * h;
      f32x16 s;
      bf16x8 pf[2];
      bf16x8 kf[8], vf[8];

      if (kh == 0) {
        pre(p0);
        load_k(KST(p0), kbase, kf);
        qk_mfma(kf, qf, s);
        softmax_tile(s, p0 == pm, key0, len, o, m_run, l_run, pf);
        post(p0);
        for (int p = p0 + 1; p < p1; ++p) {
          pre(p);
          load_v(VST(p - 1), vbase, vf);
          load_k(KST(p), kbase, kf);
          __builtin_amdgcn_sched_barrier(0);
          pv_mfma(vf, pf, o);
          qk_mfma(kf, qf, s);
          softmax_tile(s, p == pm, key0, len, o, m_run, l_run, pf);
          post(p);
        }
        load_v(VST(p1 - 1), vbase, vf);
        pv_mfma(vf, pf, o);
      } else {
        pre(p0);
        load_k(KST(p0), kbase, kf);
        qk_mfma(kf, qf, s);
        post(p0);
        for (int p = p0 + 1; p < p1; ++p) {
          pre(p);
          load_v(VST(p - 1), vbase, vf);
          load_k(KST(p), kbase, kf);
          __builtin_amdgcn_sched_barrier(0);
          softmax_tile(s, false, key0, len, o, m_run, l_run, pf);
          __builtin_amdgcn_sched_barrier(0);
          pv_mfma(vf, pf, o);
          qk_mfma(kf, qf, s);
          post(p);
        }
        load_v(VST(p1 - 1), vbase, vf);
        softmax_tile(s, p1 - 1 == pm, key0, len, o, m_run, l_run, pf);
        pv_mfma(vf, pf, o);
      }
      load_q_issue(min(b + 1, B - 1));
      {
        auto rr = __builtin_amdgcn_permlane32_swap(__float_as_uint(l_run), __float_as_uint(l_run), false, false);
        l_run = __uint_as_float(rr[0]) + __uint_as_float(rr[1]);
      }
      float* lo_ = (float*)(rg < 2 ? lds + NST * STAGE + rg * 16384 : (rg == 2 ? KST(p1 + 3) : VST(p1 + 2)));
      float* lst = (float*)VST(p1 + 3) + rg * 64;
      BAR();
      if (kh == 1) {
#pragma unroll
        for (int c = 0; c < 4; ++c)
#pragma unroll
          for (int g = 0; g < 4; ++g)
            *(f32x4*)(lo_ + ((c * 4 + g) * 64 + lane) * 4) =
                f32x4{o[c][4 * g], o[c][4 * g + 1], o[c][4 * g + 2], o[c][4 * g + 3]};
        if (h == 0) { lst[l31 * 2] = m_run; lst[l31 * 2 + 1] = l_run; }
      }
      BAR();
      if (kh == 0) {
        const float m1 = lst[l31 * 2], l1 = lst[l31 * 2 + 1];
        const float mn = fmaxf(m_run, m1);
        const float a0 = (mn == -__builtin_inff()) ? 0.f : __builtin_amdgcn_exp2f((m_run - mn) * SCALE_L2E);
        const float a1 = (mn == -__builtin_inff()) ? 0.f : __builtin_amdgcn_exp2f((m1 - mn) * SCALE_L2E);
        float* pod = po + (size_t)slot * (4 * 4096) + rg * 4096;
#pragma unroll
        for (int c = 0; c < 4; ++c)
#pragma unroll
          for (int g = 0; g < 4; ++g) {
            const f32x4 x = *(const f32x4*)(lo_ + ((c * 4 + g) * 64 + lane) * 4);
            f32x4 y;
#pragma unroll
            for (int e = 0; e < 4; ++e) y[e] = o[c][4 * g + e] * a0 + x[e] * a1;
            *(f32x4*)(pod + ((c * 4 + g) * 64 + lane) * 4) = y;
          }
        if (h == 0) {
          float* pm_ = pml + ((size_t)slot * 4 + rg) * 64 + l31 * 2;
          pm_[0] = mn; pm_[1] = l_run * a0 + l1 * a1;
        }
      }
      BAR();
      load_q_wait();
    }
    base += n;
  }
}

__global__ __launch_bounds__(256) void combine_kernel(const int* __restrict__ cl, const float* __restrict__ po,
                                                      const float* __restrict__ pml, __hip_bfloat16* __restrict__ out) {
  const int s = blockIdx.x >> 2, rg = blockIdx.x & 3;
  const int b = s / HKV, kvh = s % HKV;
  const int tid = threadIdx.x, lane = tid & 63, c = tid >> 6;
  const int l31 = lane & 31, h = lane >> 5;
  int total = 0, base = 0, n = 0;
  for (int i = 0; i < B; ++i) {
    const int ni = (eff_len(cl[i]) + KT - 1) / KT;
    if (i < b) base += ni;
    if (i == b) n = ni;
    total += ni;
  }
  const int x0 = base, x1 = base + n - 1;
  const int wf = ((x0 + 1) * NGRP + total - 1) / total - 1;
  const int wl = ((x1 + 1) * NGRP + total - 1) / total - 1;
  float M = -__builtin_inff();
  for (int w = wf; w <= wl; ++w) M = fmaxf(M, pml[((size_t)((w + b) * HKV + kvh) * 4 + rg) * 64 + l31 * 2]);
  float L = 0.f;
  f32x4 acc[4];
#pragma unroll
  for (int g = 0; g < 4; ++g) acc[g] = f32x4{0.f, 0.f, 0.f, 0.f};
  for (int w = wf; w <= wl; ++w) {
    const int slot = (w + b) * HKV + kvh;
    const float m = pml[((size_t)slot * 4 + rg) * 64 + l31 * 2];
    const float l = pml[((size_t)slot * 4 + rg) * 64 + l31 * 2 + 1];
    const float a = (m == -__builtin_inff()) ? 0.f : __builtin_amdgcn_exp2f((m - M) * SCALE_L2E);
    L += a * l;
    const float* p = po + (size_t)slot * (4 * 4096) + rg * 4096;
#pragma unroll
    for (int g = 0; g < 4; ++g) {
      const f32x4 x = *(const f32x4*)(p + ((c * 4 + g) * 64 + lane) * 4);
#pragma unroll
      for (int e = 0; e < 4; ++e) acc[g][e] += a * x[e];
    }
  }
  const float inv = 1.f / L;
  __hip_bfloat16* op = out + ((size_t)((b * HQ + kvh * G + rg) * SQ + l31)) * D + 32 * c + 4 * h;
#pragma unroll
  for (int g = 0; g < 4; ++g) {
    u32x2 v = {cvtpk(acc[g][0] * inv, acc[g][1] * inv), cvtpk(acc[g][2] * inv, acc[g][3] * inv)};
    *(u32x2*)(op + 8 * g) = v;
  }
}

extern "C" void kernel_launch(void* const* d_in, const int* in_sizes, int n_in, void* d_out, int out_size, void* d_ws,
                              size_t ws_size, hipStream_t stream) {
  const __hip_bfloat16* q = (const __hip_bfloat16*)d_in[0];
  const __hip_bfloat16* kc = (const __hip_bfloat16*)d_in[1];
  const __hip_bfloat16* vc = (const __hip_bfloat16*)d_in[2];
  const int* bt = (const int*)d_in[3];
  const int* cl = (const int*)d_in[4];
  float* po = (float*)d_ws;
  float* pml = po + (size_t)NSLOT * 4 * 4096;
  const int shm = NST * STAGE + 32768;
  hipFuncSetAttribute((const void*)attn_kernel, hipFuncAttributeMaxDynamicSharedMemorySize, shm);
  attn_kernel<<<NWG, 512, shm, stream>>>(q, kc, vc, bt, cl, po, pml);
  combine_kernel<<<B * HKV * G, 256, 0, stream>>>(cl, po, pml, (__hip_bfloat16*)d_out);
}
```

```cpp
#include <hip/hip_runtime.h>
#include <hip/hip_bf16.h>
#include <stdint.h>

typedef __attribute__((ext_vector_type(8))) short bf16x8;
typedef __attribute__((ext_vector_type(4))) short s16x4;
typedef __attribute__((ext_vector_type(16))) float f32x16;
typedef __attribute__((ext_vector_type(4))) float f32x4;
typedef __attribute__((ext_vector_type(4))) unsigned u32x4;
typedef __attribute__((ext_vector_type(2))) unsigned u32x2;

constexpr int B = 16, HQ = 32, HKV = 8, SQ = 32, D = 128, BS = 16, NBPS = 256, SKV = NBPS * BS, G = HQ / HKV;
constexpr int KT = 64;
constexpr int NST = 4;
constexpr int NWG = 256;
constexpr int STAGE = 32768;
constexpr int NGRP = NWG / HKV;
constexpr int NSLOT = (NGRP + B) * HKV;
constexpr float SCALE_L2E = 0.088388347648318447f * 1.4426950408889634f;
constexpr float THR = 8.0f / (0.088388347648318447f * 1.4426950408889634f);

__device__ __forceinline__ int crow(int r, int h) { return (r & 3) + 8 * (r >> 2) + 4 * h; }
__device__ __forceinline__ unsigned cvtpk(float lo, float hi) {
  unsigned r; asm("v_cvt_pk_bf16_f32 %0, %1, %2" : "=v"(r) : "v"(lo), "v"(hi)); return r;
}
__device__ __forceinline__ int eff_len(int cl) { return (cl <= 0 || cl > SKV) ? SKV : cl; }
constexpr int SEAM = 3;
__device__ __forceinline__ int split_pos(int g, const int* __restrict__ cl) {
  int V = SEAM * (B - 1);
  for (int b = 0; b < B; ++b) V += (eff_len(cl[b]) + KT - 1) / KT;
  const int v = (g * V) / NGRP;
  int vb = 0, cum = 0, pos = 0;
  for (int b = 0; b < B; ++b) {
    const int n = (eff_len(cl[b]) + KT - 1) / KT;
    if (b > 0) vb += SEAM;
    if (v >= vb) pos = cum + min(v - vb, n);
    else if (v >= vb - SEAM) pos = cum;
    vb += n; cum += n;
  }
  return pos;
}

#define DMA_POLICY "nt"
__device__ __forceinline__ void dma16(unsigned lds_addr, unsigned voff, const void* sbase) {
  unsigned keep;
  asm volatile("s_mov_b32 %0, m0\n\ts_mov_b32 m0, %1\n\ts_nop 0\n\tglobal_load_lds_dwordx4 %2, %3 " DMA_POLICY "\n\ts_mov_b32 m0, %0"
               : "=&s"(keep) : "s"(lds_addr), "v"(voff), "s"(sbase) : "memory");
}

__device__ __forceinline__ void load_k(const char* Ks, unsigned kbase, bf16x8 (&kf)[8]) {
#pragma unroll
  for (int d0 = 0; d0 < 8; ++d0) kf[d0] = *(const bf16x8*)(Ks + (kbase ^ (unsigned)(d0 << 5)));
}
__device__ __forceinline__ void load_v(const char* Vs, const unsigned (&vbase)[2], bf16x8 (&vf)[8]) {
#pragma unroll
  for (int c = 0; c < 4; ++c)
#pragma unroll
    for (int ks = 0; ks < 2; ++ks) {
      const s16x4 a = __builtin_amdgcn_ds_read_tr16_b64_v4i16(
          (__attribute__((address_space(3))) s16x4*)(Vs + ((vbase[0] ^ (unsigned)(c << 6)) + 4096 * ks)));
      const s16x4 b = __builtin_amdgcn_ds_read_tr16_b64_v4i16(
          (__attribute__((address_space(3))) s16x4*)(Vs + ((vbase[1] ^ (unsigned)(c << 6)) + 4096 * ks)));
      vf[c * 2 + ks] = __builtin_shufflevector(a, b, 0, 1, 2, 3, 4, 5, 6, 7);
    }
}
__device__ __forceinline__ void qk_mfma(const bf16x8 (&kf)[8], const bf16x8 (&qf)[8], f32x16& s) {
#pragma unroll
  for (int r = 0; r < 16; ++r) s[r] = 0.f;
#pragma unroll
  for (int d0 = 0; d0 < 8; ++d0) s = __builtin_amdgcn_mfma_f32_32x32x16_bf16(kf[d0], qf[d0], s, 0, 0, 0);
}
__device__ __forceinline__ void pv_mfma(const bf16x8 (&vf)[8], const bf16x8 (&pf)[2], f32x16 (&o)[4]) {
#pragma unroll
  for (int ks = 0; ks < 2; ++ks)
#pragma unroll
    for (int c = 0; c < 4; ++c) o[c] = __builtin_amdgcn_mfma_f32_32x32x16_bf16(vf[c * 2 + ks], pf[ks], o[c], 0, 0, 0);
}
__device__ __forceinline__ void softmax_tile(f32x16& s, bool masked, int key0, int lim, f32x16 (&o)[4], float& m_run,
                                             float& l_run, bf16x8 (&pf)[2]) {
  if (masked) {
#pragma unroll
    for (int r = 0; r < 16; ++r) s[r] = (key0 + (r & 3) + 8 * (r >> 2) >= lim) ? -__builtin_inff() : s[r];
  }
  float tmax = s[0];
#pragma unroll
  for (int r = 1; r < 16; ++r) tmax = fmaxf(tmax, s[r]);
  {
    auto rr = __builtin_amdgcn_permlane32_swap(__float_as_uint(tmax), __float_as_uint(tmax), false, false);
    tmax = fmaxf(__uint_as_float(rr[0]), __uint_as_float(rr[1]));
  }
  if (__any(tmax - m_run > THR)) {
    const float mn = fmaxf(m_run, tmax);
    const float alpha = (mn == -__builtin_inff()) ? 1.f : __builtin_amdgcn_exp2f((m_run - mn) * SCALE_L2E);
    m_run = mn;
    l_run *= alpha;
#pragma unroll
    for (int c = 0; c < 4; ++c)
#pragma unroll
      for (int r = 0; r < 16; ++r) o[c][r] *= alpha;
  }
  const float msub = (m_run == -__builtin_inff()) ? 0.f : m_run * SCALE_L2E;
  float psum = 0.f;
#pragma unroll
  for (int r = 0; r < 16; ++r) {
    s[r] = __builtin_amdgcn_exp2f(fmaf(s[r], SCALE_L2E, -msub));
    psum += s[r];
  }
  l_run += psum;
#pragma unroll
  for (int ks = 0; ks < 2; ++ks) {
    u32x4 wv = {cvtpk(s[8 * ks + 0], s[8 * ks + 1]), cvtpk(s[8 * ks + 2], s[8 * ks + 3]),
                cvtpk(s[8 * ks + 4], s[8 * ks + 5]), cvtpk(s[8 * ks + 6], s[8 * ks + 7])};
    pf[ks] = __builtin_bit_cast(bf16x8, wv);
  }
}

__global__ __launch_bounds__(512, 2) void attn_kernel(const __hip_bfloat16* __restrict__ q,
                                                      const __hip_bfloat16* __restrict__ kc,
                                                      const __hip_bfloat16* __restrict__ vc,
                                                      const int* __restrict__ bt, const int* __restrict__ cl,
                                                      float* __restrict__ po, float* __restrict__ pml,
                                                      int* __restrict__ bounds) {
  extern __shared__ __attribute__((aligned(16))) char lds[];
  const int tid = threadIdx.x, lane = tid & 63;
  const int wid = __builtin_amdgcn_readfirstlane(tid >> 6);
  const int rg = wid & 3, kh = wid >> 2;
  const int l31 = lane & 31, h = lane >> 5;
  const int w = blockIdx.x;
  const unsigned ldsbase = (unsigned)(size_t)lds;

  dma16(ldsbase + NST * STAGE + wid * 2048, (unsigned)(wid * 2048 + lane * 16), bt);
  dma16(ldsbase + NST * STAGE + wid * 2048 + 1024, (unsigned)(wid * 2048 + 1024 + lane * 16), bt);

  const int grp = w >> 3, kvh = w & 7;
  int lo = split_pos(grp, cl);
  const int hi = split_pos(grp + 1, cl);
  if (kvh == 0 && tid == 0) { bounds[grp + 1] = hi; if (grp == 0) bounds[0] = lo; }

  unsigned dmaoff[2];
#pragma unroll
  for (int i = 0; i < 2; ++i) {
    const int f = ((lane >> 4) << 2) | ((2 * wid + i) & 3);
    dmaoff[i] = (unsigned)((((wid & 1) * 8 + 4 * i + (lane >> 4)) * (HKV * D * 2)) + 16 * ((lane & 15) ^ f));
  }
  const int krowi = kh * 32 + l31;
  const unsigned kbase = 256u * krowi + 16u * (unsigned)(h ^ (((krowi & 3) << 2) | ((krowi >> 2) & 3)));
  unsigned vbase[2];
  {
    const int blk = (lane >> 4) & 1, qq = (lane & 15) >> 2, pp = lane & 3;
#pragma unroll
    for (int tt = 0; tt < 2; ++tt) {
      const int row = kh * 32 + 8 * tt + 4 * h + qq;
      const int ch = 2 * blk + (pp >> 1);
      vbase[tt] = 256u * row + 16u * (unsigned)(ch ^ (((row & 3) << 2) | ((row >> 2) & 3))) + 8u * (pp & 1);
    }
  }

  const int N = hi - lo;
  if (N <= 0) return;
  int mypage;
  {
    const int pos = lo + lane;
    int bsel = 0, bbase = 0, base = 0;
    for (int b = 0; b < B; ++b) {
      const int n = (eff_len(cl[b]) + KT - 1) / KT;
      base += n;
      if (pos >= base && b + 1 < B) { bsel = b + 1; bbase = base; }
    }
    asm volatile("s_waitcnt vmcnt(0)" ::: "memory");
    __builtin_amdgcn_s_barrier();
    mypage = ((const int*)(lds + NST * STAGE))[bsel * NBPS + 4 * min(pos - bbase, SKV / KT - 1) + (wid >> 1)];
  }
  const char* kbp = (const char*)kc + kvh * (D * 2);
  const char* vbp = (const char*)vc + kvh * (D * 2);
  auto issueK = [&](int p) {
    const int phys = __builtin_amdgcn_readlane(mypage, p);
    const size_t pg = (size_t)phys * (BS * HKV * D * 2);
    const unsigned dst = ldsbase + (p & 3) * STAGE + wid * 2048;
    dma16(dst, dmaoff[0], kbp + pg);
    dma16(dst + 1024, dmaoff[1], kbp + pg);
  };
  auto issueV = [&](int p) {
    const int phys = __builtin_amdgcn_readlane(mypage, p);
    const size_t pg = (size_t)phys * (BS * HKV * D * 2);
    const unsigned dst = ldsbase + (p & 3) * STAGE + wid * 2048 + 16384;
    dma16(dst, dmaoff[0], vbp + pg);
    dma16(dst + 1024, dmaoff[1], vbp + pg);
  };
  auto pre = [&](int p) {
    if (p + 3 < N) issueK(p + 3);
    if (p + 2 < N) issueV(p + 2);
  };
  auto post = [&](int p) {
    if (p + 3 < N) asm volatile("s_waitcnt vmcnt(8)" ::: "memory");
    else if (p + 2 < N) asm volatile("s_waitcnt vmcnt(6)" ::: "memory");
    else asm volatile("s_waitcnt vmcnt(0)" ::: "memory");
    __builtin_amdgcn_s_barrier();
  };
#define KST(p) (lds + ((p) & 3) * STAGE)
#define VST(p) (lds + ((p) & 3) * STAGE + 16384)
#define BAR() do { asm volatile("s_waitcnt lgkmcnt(0)" ::: "memory"); __builtin_amdgcn_s_barrier(); } while (0)

  bf16x8 qf[8];
  auto load_q_plain = [&](int b) {
    const __hip_bfloat16* qp = q + ((size_t)((b * HQ + kvh * G + rg) * SQ + l31)) * D + 8 * h;
#pragma unroll
    for (int d0 = 0; d0 < 8; ++d0) qf[d0] = *(const bf16x8*)(qp + 16 * d0);
  };
  auto load_q_issue = [&](int b) {
    const __hip_bfloat16* qp = q + ((size_t)((b * HQ + kvh * G + rg) * SQ + l31)) * D + 8 * h;
#pragma unroll
    for (int d0 = 0; d0 < 8; ++d0)
      asm volatile("global_load_dwordx4 %0, %1, off offset:%2" : "+v"(qf[d0]) : "v"(qp), "i"(32 * d0) : "memory");
  };
  auto load_q_wait = [&]() {
    asm volatile("s_waitcnt vmcnt(0)"
                 : "+v"(qf[0]), "+v"(qf[1]), "+v"(qf[2]), "+v"(qf[3]), "+v"(qf[4]), "+v"(qf[5]), "+v"(qf[6]), "+v"(qf[7])
                 :: "memory");
  };

  int first_b = 0;
  {
    int base = 0;
    for (int b = 0; b < B; ++b) { base += (eff_len(cl[b]) + KT - 1) / KT; if (lo >= base) first_b = b + 1; }
  }
#pragma unroll
  for (int d0 = 0; d0 < 8; ++d0) qf[d0] = bf16x8{0, 0, 0, 0, 0, 0, 0, 0};
  load_q_issue(first_b);
  issueK(0); issueV(0);
  if (N > 1) { issueK(1); issueV(1); }
  if (N > 2) issueK(2);
#define QWAIT(NN) asm volatile("s_waitcnt vmcnt(" #NN ")" : "+v"(qf[0]), "+v"(qf[1]), "+v"(qf[2]), "+v"(qf[3]), "+v"(qf[4]), "+v"(qf[5]), "+v"(qf[6]), "+v"(qf[7]) :: "memory")
  QWAIT(0);

  int base = 0, pos = lo;
  for (int b = 0; b < B && pos < hi; ++b) {
    const int clb = cl[b];
    const int len = eff_len(clb);
    const bool allmask = clb <= 0;
    const int n = (len + KT - 1) / KT;
    if (pos < base + n) {
      const int t0 = pos - base;
      const int t1 = min(n, t0 + (hi - pos));
      const int slot = (grp + b) * HKV + kvh;
      const int p0 = pos - lo, p1 = p0 + (t1 - t0);
      pos += t1 - t0;

      __builtin_amdgcn_s_barrier();
      if (allmask) {
#pragma unroll
        for (int d0 = 0; d0 < 8; ++d0) qf[d0] = bf16x8{0, 0, 0, 0, 0, 0, 0, 0};
      }
      f32x16 o[4];
#pragma unroll
      for (int c = 0; c < 4; ++c)
#pragma unroll
        for (int r = 0; r < 16; ++r) o[c][r] = 0.f;
      float m_run = -__builtin_inff(), l_run = 0.f;
      const int pm = p0 + (n - 1 - t0);
      const int key0 = (n - 1) * KT + kh * 32 + 4 * h;
      f32x16 s;
      bf16x8 pf[2];
      bf16x8 kf[8], vf[8];

      if (kh == 0) {
        pre(p0);
        load_k(KST(p0), kbase, kf);
        qk_mfma(kf, qf, s);
        softmax_tile(s, p0 == pm, key0, len, o, m_run, l_run, pf);
        post(p0);
        for (int p = p0 + 1; p < p1; ++p) {
          pre(p);
          load_v(VST(p - 1), vbase, vf);
          load_k(KST(p), kbase, kf);
          __builtin_amdgcn_sched_barrier(0);
          pv_mfma(vf, pf, o);
          qk_mfma(kf, qf, s);
          softmax_tile(s, p == pm, key0, len, o, m_run, l_run, pf);
          post(p);
        }
        load_v(VST(p1 - 1), vbase, vf);
        pv_mfma(vf, pf, o);
      } else {
        pre(p0);
        load_k(KST(p0), kbase, kf);
        qk_mfma(kf, qf, s);
        post(p0);
        for (int p = p0 + 1; p < p1; ++p) {
          pre(p);
          load_v(VST(p - 1), vbase, vf);
          load_k(KST(p), kbase, kf);
          __builtin_amdgcn_sched_barrier(0);
          softmax_tile(s, false, key0, len, o, m_run, l_run, pf);
          __builtin_amdgcn_sched_barrier(0);
          pv_mfma(vf, pf, o);
          qk_mfma(kf, qf, s);
          post(p);
        }
        load_v(VST(p1 - 1), vbase, vf);
        softmax_tile(s, p1 - 1 == pm, key0, len, o, m_run, l_run, pf);
        pv_mfma(vf, pf, o);
      }
      load_q_issue(min(b + 1, B - 1));
      {
        auto rr = __builtin_amdgcn_permlane32_swap(__float_as_uint(l_run), __float_as_uint(l_run), false, false);
        l_run = __uint_as_float(rr[0]) + __uint_as_float(rr[1]);
      }
      float* lo_ = (float*)(rg < 2 ? lds + NST * STAGE + rg * 16384 : (rg == 2 ? KST(p1 + 3) : VST(p1 + 2)));
      float* lst = (float*)VST(p1 + 3) + rg * 64;
      BAR();
      if (kh == 1) {
#pragma unroll
        for (int c = 0; c < 4; ++c)
#pragma unroll
          for (int g = 0; g < 4; ++g)
            *(f32x4*)(lo_ + ((c * 4 + g) * 64 + lane) * 4) =
                f32x4{o[c][4 * g], o[c][4 * g + 1], o[c][4 * g + 2], o[c][4 * g + 3]};
        if (h == 0) { lst[l31 * 2] = m_run; lst[l31 * 2 + 1] = l_run; }
      }
      BAR();
      if (kh == 0) {
        const float m1 = lst[l31 * 2], l1 = lst[l31 * 2 + 1];
        const float mn = fmaxf(m_run, m1);
        const float a0 = (mn == -__builtin_inff()) ? 0.f : __builtin_amdgcn_exp2f((m_run - mn) * SCALE_L2E);
        const float a1 = (mn == -__builtin_inff()) ? 0.f : __builtin_amdgcn_exp2f((m1 - mn) * SCALE_L2E);
        unsigned short* pod = (unsigned short*)po + (size_t)slot * (4 * 4096) + rg * 4096;
#pragma unroll
        for (int c = 0; c < 4; ++c)
#pragma unroll
          for (int gp = 0; gp < 2; ++gp) {
            const f32x4 x0 = *(const f32x4*)(lo_ + ((c * 4 + 2 * gp) * 64 + lane) * 4);
            const f32x4 x1 = *(const f32x4*)(lo_ + ((c * 4 + 2 * gp + 1) * 64 + lane) * 4);
            const int r0 = 8 * gp;
            u32x4 y = {cvtpk(o[c][r0 + 0] * a0 + x0[0] * a1, o[c][r0 + 1] * a0 + x0[1] * a1),
                       cvtpk(o[c][r0 + 2] * a0 + x0[2] * a1, o[c][r0 + 3] * a0 + x0[3] * a1),
                       cvtpk(o[c][r0 + 4] * a0 + x1[0] * a1, o[c][r0 + 5] * a0 + x1[1] * a1),
                       cvtpk(o[c][r0 + 6] * a0 + x1[2] * a1, o[c][r0 + 7] * a0 + x1[3] * a1)};
            *(u32x4*)(pod + ((c * 2 + gp) * 64 + lane) * 8) = y;
          }
        if (h == 0) {
          float* pm_ = pml + ((size_t)slot * 4 + rg) * 64 + l31 * 2;
          pm_[0] = mn; pm_[1] = l_run * a0 + l1 * a1;
        }
      }
      BAR();
      load_q_wait();
    }
    base += n;
  }
}

__global__ __launch_bounds__(256) void combine_kernel(const int* __restrict__ cl, const float* __restrict__ po,
                                                      const float* __restrict__ pml, const int* __restrict__ bounds,
                                                      __hip_bfloat16* __restrict__ out) {
  const int s = blockIdx.x >> 2, rg = blockIdx.x & 3;
  const int b = s / HKV, kvh = s % HKV;
  const int tid = threadIdx.x, lane = tid & 63, c = tid >> 6;
  const int l31 = lane & 31, h = lane >> 5;
  const int bl = bounds[l31], bh = bounds[l31 + 1];
  const int myn = (eff_len(cl[lane & 15]) + KT - 1) / KT;
  int base = 0, n = 0;
#pragma unroll
  for (int i = 0; i < B; ++i) {
    const int ni = __builtin_amdgcn_readlane(myn, i);
    if (i < b) base += ni;
    if (i == b) n = ni;
  }
  const unsigned long long hit = __ballot(lane < 32 && bl < bh && bl < base + n && bh > base);
  const int wf = __builtin_ctzll(hit), wl = 63 - __builtin_clzll(hit);

  float M = -__builtin_inff(), L = 0.f;
  f32x4 acc[4];
#pragma unroll
  for (int g = 0; g < 4; ++g) acc[g] = f32x4{0.f, 0.f, 0.f, 0.f};
  for (int w0 = wf; w0 <= wl; w0 += 4) {
    float m[4], l[4];
    u32x4 x[4][2];
#pragma unroll
    for (int i = 0; i < 4; ++i) {
      const int slot = (min(w0 + i, wl) + b) * HKV + kvh;
      m[i] = pml[((size_t)slot * 4 + rg) * 64 + l31 * 2];
      l[i] = pml[((size_t)slot * 4 + rg) * 64 + l31 * 2 + 1];
      const unsigned short* p = (const unsigned short*)po + (size_t)slot * (4 * 4096) + rg * 4096;
      x[i][0] = *(const u32x4*)(p + ((c * 2 + 0) * 64 + lane) * 8);
      x[i][1] = *(const u32x4*)(p + ((c * 2 + 1) * 64 + lane) * 8);
    }
    float Mn = M;
#pragma unroll
    for (int i = 0; i < 4; ++i) { if (w0 + i > wl) m[i] = -__builtin_inff(); Mn = fmaxf(Mn, m[i]); }
    const float Ms = (Mn == -__builtin_inff()) ? 0.f : Mn;
    const float a_old = __builtin_amdgcn_exp2f((M - Ms) * SCALE_L2E);
    L *= a_old;
#pragma unroll
    for (int g = 0; g < 4; ++g)
#pragma unroll
      for (int e = 0; e < 4; ++e) acc[g][e] *= a_old;
#pragma unroll
    for (int i = 0; i < 4; ++i) {
      const float a = __builtin_amdgcn_exp2f((m[i] - Ms) * SCALE_L2E);
      L += a * l[i];
#pragma unroll
      for (int gp = 0; gp < 2; ++gp)
#pragma unroll
        for (int e = 0; e < 4; ++e) {
          acc[2 * gp + (e >> 1)][2 * (e & 1)] += a * __uint_as_float(x[i][gp][e] << 16);
          acc[2 * gp + (e >> 1)][2 * (e & 1) + 1] += a * __uint_as_float(x[i][gp][e] & 0xffff0000u);
        }
    }
    M = Mn;
  }
  const float inv = 1.f / L;
  __hip_bfloat16* op = out + ((size_t)((b * HQ + kvh * G + rg) * SQ + l31)) * D + 32 * c + 4 * h;
#pragma unroll
  for (int g = 0; g < 4; ++g) {
    u32x2 v = {cvtpk(acc[g][0] * inv, acc[g][1] * inv), cvtpk(acc[g][2] * inv, acc[g][3] * inv)};
    *(u32x2*)(op + 8 * g) = v;
  }
}

extern "C" void kernel_launch(void* const* d_in, const int* in_sizes, int n_in, void* d_out, int out_size, void* d_ws,
                              size_t ws_size, hipStream_t stream) {
  const __hip_bfloat16* q = (const __hip_bfloat16*)d_in[0];
  const __hip_bfloat16* kc = (const __hip_bfloat16*)d_in[1];
  const __hip_bfloat16* vc = (const __hip_bfloat16*)d_in[2];
  const int* bt = (const int*)d_in[3];
  const int* cl = (const int*)d_in[4];
  float* po = (float*)d_ws;
  float* pml = po + (size_t)NSLOT * 4 * 4096;
  int* bounds = (int*)(pml + (size_t)NSLOT * 4 * 64);
  const int shm = NST * STAGE + 32768;
  hipFuncSetAttribute((const void*)attn_kernel, hipFuncAttributeMaxDynamicSharedMemorySize, shm);
  attn_kernel<<<NWG, 512, shm, stream>>>(q, kc, vc, bt, cl, po, pml, bounds);
  combine_kernel<<<B * HKV * G, 256, 0, stream>>>(cl, po, pml, bounds, (__hip_bfloat16*)d_out);
}
```
